```python
import torch

def _softmax(x, axis=-1):
    return torch.softmax(x, dim=axis)
def _gelu(x):
    return torch.nn.functional.gelu(x, approximate="tanh")
def _silu(x):
    return torch.nn.functional.silu(x)
def _sigmoid(x):
    return torch.sigmoid(x)
def _layernorm(x, axis=-1, eps=1e-5):
    m = x.mean(dim=axis, keepdim=True)
    v = x.var(dim=axis, keepdim=True, unbiased=False)
    return (x - m) / torch.sqrt(v + eps)
def _scatter_add(tgt, idx, src, axis=0):
    return tgt.clone().index_add_(axis, idx, src)
def _segment_sum(src, seg, n):
    out = torch.zeros((n,) + src.shape[1:], dtype=src.dtype)
    return out.index_add_(0, seg, src)
def _pad(x, pw):
    flat = tuple(v for pair in reversed(pw) for v in pair)
    return torch.nn.functional.pad(x, flat)

kernel_name = 'parallel_sum__gqa_block_cached__glu_gated_swiglu__decode'

def setup_inputs():

    _w = (lambda a: (lambda t: t.to(torch.bfloat16) if t.is_floating_point() else t)(torch.as_tensor(a)))
    def _A_setup():
        B, Sq, D = 2, 1, 448
        Hq, Hkv, Sk, Dh = 16, 8, 32768, 256
        assert Hq % Hkv == 0, (Hq, Hkv)
        return {
            "x": _w((lambda *s: torch.randn(*s, dtype=torch.bfloat16) * 0.05)(B, Sq, D)),
            "k_cache": _w((lambda *s: torch.randn(*s, dtype=torch.bfloat16) * 3.2)(B, Hkv, Sk, Dh)),
            "v_cache": _w((lambda *s: torch.randn(*s, dtype=torch.bfloat16) * 1.6)(B, Hkv, Sk, Dh)),
            "wq": _w((lambda *s: torch.randn(*s, dtype=torch.bfloat16) * 3.2)(D, Hq * Dh) * (D ** -0.5)),
            "wo": _w((lambda *s: torch.randn(*s, dtype=torch.bfloat16) * 1.6)(Hq * Dh, D) * ((Hq * Dh) ** -0.5)),
        }
    _A_in = _A_setup()

    def _B_setup():
        B, S, D, F = 2, 1, 448, 1792
        return {
            "x": _w((lambda *s: torch.randn(*s, dtype=torch.bfloat16) * 0.05)(B, S, D)),
            "w1": _w((lambda *s: torch.randn(*s, dtype=torch.bfloat16) * 1.6)(D, F) * (2.0 / D) ** 0.5),
            "w3": _w((lambda *s: torch.randn(*s, dtype=torch.bfloat16) * 1.6)(D, F) * (2.0 / D) ** 0.5),
            "w2": _w((lambda *s: torch.randn(*s, dtype=torch.bfloat16) * 1.6)(F, D) * (2.0 / F) ** 0.5),
        }
    _B_in = _B_setup()

    return {
        "A_x": _A_in["x"],
        "A_k_cache": _A_in["k_cache"],
        "A_v_cache": _A_in["v_cache"],
        "A_wq": _A_in["wq"],
        "A_wo": _A_in["wo"],
        "B_x": _B_in["x"],
        "B_w1": _B_in["w1"],
        "B_w3": _B_in["w3"],
        "B_w2": _B_in["w2"],
    }

def reference(A_x, A_k_cache, A_v_cache, A_wq, A_wo, B_x, B_w1, B_w3, B_w2):
    def _A_ref(x, k_cache, v_cache, wq, wo):
        B, Sq, D = x.shape
        Hq, Dh = 16, 256
        Hkv = k_cache.shape[1]
        rep = Hq // Hkv
        q = (lambda x, *d: x.permute(*d))((x @ wq).reshape(B, Sq, Hq, Dh), 0, 2, 1, 3)
        k = (lambda x, n, axis: torch.repeat_interleave(x, n, dim=axis))(k_cache, rep, axis=1)
        v = (lambda x, n, axis: torch.repeat_interleave(x, n, dim=axis))(v_cache, rep, axis=1)
        scores = torch.einsum("bhqd,bhkd->bhqk", q, k) * (Dh ** -0.5)
        p = _softmax(scores, axis=-1)
        o = torch.einsum("bhqk,bhkd->bhqd", p, v)
        return (lambda x, *d: x.permute(*d))(o, 0, 2, 1, 3).reshape(B, Sq, Hq * Dh) @ wo

    def _B_ref(x, w1, w3, w2):
        gate = torch.einsum("bsd,df->bsf", x, w1)
        up = torch.einsum("bsd,df->bsf", x, w3)
        h = _silu(gate) * up
        out = torch.einsum("bsf,fd->bsd", h, w2)
        return out

    x = A_x
    xn = _layernorm(x, axis=-1)
    return x + _A_ref(xn, A_k_cache, A_v_cache, A_wq, A_wo) + _B_ref(xn, B_w1, B_w3, B_w2)
```

```cpp
#include <hip/hip_runtime.h>
#include <hip/hip_bf16.h>
#include <stdint.h>

typedef unsigned u32;
typedef unsigned short u16;
typedef unsigned u32x4 __attribute__((ext_vector_type(4)));
typedef unsigned u32x2 __attribute__((ext_vector_type(2)));
typedef float f32x4 __attribute__((ext_vector_type(4)));
typedef float f32x2 __attribute__((ext_vector_type(2)));
typedef __bf16 bf16x8 __attribute__((ext_vector_type(8)));
typedef __bf16 bf16x2 __attribute__((ext_vector_type(2)));
typedef __amdgpu_buffer_rsrc_t rsrc_t;

#define ROUND_SCORES 1

constexpr int DM = 448, HKV = 8, SK = 32768, DH = 256, FF = 1792, QN = 4096;
constexpr int NWS = 4;
constexpr int NTHR = (NWS + 1) * 64;
constexpr int NWG = 256;
constexpr int TILES_WG = 128;
constexpr int TPW = TILES_WG / NWS;
constexpr u32 SPIN_CAP = 1u << 16;
constexpr float SC2 = 0.0625f * 1.4426950408889634f;

constexpr int WS_E = 0;
constexpr int WS_Q = 4096;
constexpr int WS_ML = WS_Q + 2 * 8 * 2048;
constexpr int WS_P2 = WS_ML + 16 * 16 * 16 * 16;
constexpr int WS_P3 = WS_P2 + 16 * 16 * 16 * 256;
constexpr int WS_P4 = WS_P3 + 2 * 112 * 128 * 32;
constexpr int WS_END = WS_P4 + 2 * 112 * 128 * 32;

constexpr int L_XN = 0;
constexpr int L_Q = 2048;
constexpr int L_TAG = 3072;
constexpr int L_ML = 3328;
constexpr int L_O = 4096;
constexpr int L_BYTES = 96 * 1024;

struct Params {
  const u16* x; const char* K; const char* V; const u16* wq; const u16* wo;
  const u16* w1; const u16* w3; const u16* w2; u16* out; char* ws;
};

__device__ __forceinline__ float lo16(u32 v) { return __builtin_bit_cast(float, v << 16); }
__device__ __forceinline__ float hi16(u32 v) { return __builtin_bit_cast(float, v & 0xffff0000u); }
__device__ __forceinline__ u32 pk2(float a, float b) { f32x2 v = {a, b}; return __builtin_bit_cast(u32, __builtin_convertvector(v, bf16x2)); }
__device__ __forceinline__ float rbf(float a) { return lo16(pk2(a, a)); }
__device__ __forceinline__ u32 fbits(float a) { return __builtin_bit_cast(u32, a); }
__device__ __forceinline__ float bitsf(u32 a) { return __builtin_bit_cast(float, a); }
__device__ __forceinline__ float dot2(u32 a, u32 b, float c) {
  return __builtin_amdgcn_fdot2_f32_bf16(__builtin_bit_cast(bf16x2, a), __builtin_bit_cast(bf16x2, b), c, false);
}
__device__ __forceinline__ f32x4 mfma(u32x4 a, u32x4 b, f32x4 c) {
  return __builtin_amdgcn_mfma_f32_16x16x32_bf16(__builtin_bit_cast(bf16x8, a), __builtin_bit_cast(bf16x8, b), c, 0, 0, 0);
}
template <int CTRL> __device__ __forceinline__ float dpp(float x) {
  return __builtin_bit_cast(float, __builtin_amdgcn_mov_dpp(__builtin_bit_cast(int, x), CTRL, 0xf, 0xf, true));
}
constexpr int ROR1 = 0x121, ROR2 = 0x122, ROR4 = 0x124, ROR8 = 0x128;
__device__ __forceinline__ float row_sum(float x) { x += dpp<ROR8>(x); x += dpp<ROR4>(x); x += dpp<ROR2>(x); x += dpp<ROR1>(x); return x; }
__device__ __forceinline__ float row_max(float x) { x = fmaxf(x, dpp<ROR8>(x)); x = fmaxf(x, dpp<ROR4>(x)); x = fmaxf(x, dpp<ROR2>(x)); x = fmaxf(x, dpp<ROR1>(x)); return x; }
__device__ __forceinline__ float swap16_add(float a, float b) {
  auto s = __builtin_amdgcn_permlane16_swap(fbits(a), fbits(b), false, false); return bitsf(s[0]) + bitsf(s[1]);
}
__device__ __forceinline__ float swap32_add(float a, float b) {
  auto s = __builtin_amdgcn_permlane32_swap(fbits(a), fbits(b), false, false); return bitsf(s[0]) + bitsf(s[1]);
}
__device__ __forceinline__ float xrow_sum(float x) { x = swap16_add(x, x); return swap32_add(x, x); }
__device__ __forceinline__ float xrow_max(float x) {
  auto s = __builtin_amdgcn_permlane16_swap(fbits(x), fbits(x), false, false); x = fmaxf(bitsf(s[0]), bitsf(s[1]));
  auto t = __builtin_amdgcn_permlane32_swap(fbits(x), fbits(x), false, false); return fmaxf(bitsf(t[0]), bitsf(t[1]));
}
__device__ __forceinline__ rsrc_t mkrsrc(const void* p, int bytes) { return __builtin_amdgcn_make_buffer_rsrc((void*)p, 0, bytes, 0x00020000); }
__device__ __forceinline__ u32x4 bload(rsrc_t r, int voff, int soff) { return __builtin_amdgcn_raw_buffer_load_b128(r, voff, soff, 0); }
__device__ __forceinline__ void st_sc1(u32x4 v, rsrc_t r, int voff) { __builtin_amdgcn_raw_buffer_store_b128(v, r, voff, 0, 16); }
__device__ __forceinline__ void wg_barrier() {
  asm volatile("s_waitcnt lgkmcnt(0)\n\ts_barrier" ::: "memory");
}
__device__ __forceinline__ void poll1(u32x4& a, rsrc_t r, int voff) {
  asm volatile("buffer_load_dwordx4 %0, %1, %2, 0 offen sc1\n\ts_waitcnt vmcnt(0)" : "=&v"(a) : "v"(voff), "s"(r) : "memory");
}
__device__ __forceinline__ void poll2(u32x4& a, u32x4& b, rsrc_t r, int voff) {
  asm volatile("buffer_load_dwordx4 %0, %2, %3, 0 offen sc1\n\tbuffer_load_dwordx4 %1, %2, %3, 0 offen offset:1024 sc1\n\ts_waitcnt vmcnt(0)"
               : "=&v"(a), "=&v"(b) : "v"(voff), "s"(r) : "memory");
}
template <int STEP> __device__ __forceinline__ void poll4(u32x4 (&a)[4], rsrc_t r, int voff) {
  asm volatile("buffer_load_dwordx4 %0, %4, %5, 0 offen sc1\n\tbuffer_load_dwordx4 %1, %4, %5, 0 offen offset:%6 sc1\n\t"
               "buffer_load_dwordx4 %2, %4, %5, 0 offen offset:%7 sc1\n\tbuffer_load_dwordx4 %3, %4, %5, 0 offen offset:%8 sc1\n\ts_waitcnt vmcnt(0)"
               : "=&v"(a[0]), "=&v"(a[1]), "=&v"(a[2]), "=&v"(a[3]) : "v"(voff), "s"(r), "i"(STEP), "i"(2 * STEP), "i"(3 * STEP) : "memory");
}
__device__ __forceinline__ bool tags_ok(u32x4 a, u32 tag) { return a.y == tag && a.w == tag; }

__device__ __forceinline__ void gemv_pairs(const u32x4 (&w)[14], const u32 (&xp)[7], float* acc) {
#pragma unroll
  for (int i = 0; i < 7; ++i)
#pragma unroll
    for (int u = 0; u < 4; ++u) {
      const u32 lo = __builtin_amdgcn_perm(w[2 * i + 1][u], w[2 * i][u], 0x05040100u);
      const u32 hi = __builtin_amdgcn_perm(w[2 * i + 1][u], w[2 * i][u], 0x07060302u);
      acc[2 * u] = dot2(lo, xp[i], acc[2 * u]);
      acc[2 * u + 1] = dot2(hi, xp[i], acc[2 * u + 1]);
    }
}
__device__ __forceinline__ void reduce16(const float (&v)[16], float (&o)[4]) {
  float a[8];
#pragma unroll
  for (int i = 0; i < 8; ++i) a[i] = swap32_add(v[i], v[i + 8]);
#pragma unroll
  for (int i = 0; i < 4; ++i) { float t = swap16_add(a[i], a[i + 4]); t += dpp<ROR8>(t); t += dpp<ROR4>(t); t += dpp<ROR2>(t); o[i] = t; }
}
template <int NP> __device__ __forceinline__ void gather4(rsrc_t rws, int base, int lane, u32 tag, float& s0, float& s1) {
  u32x4 g[4];
  for (u32 spins = 0; spins < SPIN_CAP; ++spins) {
    poll4<1024>(g, rws, base + lane * 16);
    bool ok = tags_ok(g[0], tag) && tags_ok(g[1], tag) && tags_ok(g[2], tag) && (tags_ok(g[3], tag) || (lane >> 1) + 96 >= NP);
    if (__all(ok)) break;
    __builtin_amdgcn_s_sleep(2);
  }
  const bool v3 = (lane >> 1) + 96 < NP;
  float a = (bitsf(g[0].x) + bitsf(g[1].x)) + (bitsf(g[2].x) + (v3 ? bitsf(g[3].x) : 0.f));
  float b = (bitsf(g[0].z) + bitsf(g[1].z)) + (bitsf(g[2].z) + (v3 ? bitsf(g[3].z) : 0.f));
  a += dpp<ROR8>(a); a += dpp<ROR4>(a); a += dpp<ROR2>(a);
  b += dpp<ROR8>(b); b += dpp<ROR4>(b); b += dpp<ROR2>(b);
  s0 = xrow_sum(a); s1 = xrow_sum(b);
}

__global__ void __launch_bounds__(NTHR) fused_decode_kernel(Params p) {
  __shared__ __attribute__((aligned(16))) char lds[L_BYTES];
  const int tid = threadIdx.x, lane = tid & 63, wave = __builtin_amdgcn_readfirstlane(tid >> 6);
  const int bid = blockIdx.x, xl = bid & 7, jj = bid >> 3;
  const int s = xl * 2 + (jj & 1), kvh = (jj >> 1) & 7, b = jj >> 4, grp = b * 8 + kvh;
  const int g = lane >> 4, c = lane & 15;
  const rsrc_t rws = mkrsrc(p.ws, WS_END);
  const int eslot = (c & 3) | ((c >> 3) << 2);
  u32 msk[4];
#pragma unroll
  for (int u = 0; u < 4; ++u) msk[u] = ((eslot >> 1) == u) ? ((eslot & 1) ? 0xffff0000u : 0x0000ffffu) : 0u;

  if (wave == 0) {
    __builtin_amdgcn_s_setprio(3);
    const int rg = lane >> 1, half = lane & 1;
    const rsrc_t rx = mkrsrc(p.x, 2 * DM * 2);
    const u32x4 xr0 = bload(rx, lane * 16, 0), xr1 = bload(rx, lane * 16, DM * 2);
    const u32 tag = __builtin_amdgcn_readfirstlane(__builtin_amdgcn_raw_buffer_load_b32(rws, WS_E, 0, 16)) + 1u;
    u32x4 w[14];
    {
      const rsrc_t rq = mkrsrc(p.wq, DM * QN * 2);
      const int vo = rg * 2 * (QN * 2) + (xl * 512 + jj * 16 + half * 8) * 2;
#pragma unroll
      for (int i = 0; i < 7; ++i) { w[2 * i] = bload(rq, vo, i * 64 * QN * 2); w[2 * i + 1] = bload(rq, vo + QN * 2, i * 64 * QN * 2); }
    }
    u32x4 xn[2];
#pragma unroll
    for (int r = 0; r < 2; ++r) {
      const u32x4 xr = r ? xr1 : xr0;
      float e[8];
#pragma unroll
      for (int u = 0; u < 4; ++u) { e[2 * u] = lane < 56 ? lo16(xr[u]) : 0.f; e[2 * u + 1] = lane < 56 ? hi16(xr[u]) : 0.f; }
      float sm = ((e[0] + e[1]) + (e[2] + e[3])) + ((e[4] + e[5]) + (e[6] + e[7]));
      const float mean = xrow_sum(row_sum(sm)) * (1.0f / DM);
      float sq = 0.f;
#pragma unroll
      for (int k = 0; k < 8; ++k) { const float d = e[k] - mean; sq = fmaf(d, d, sq); }
      sq = lane < 56 ? sq : 0.f;
      const float var = xrow_sum(row_sum(sq)) * (1.0f / DM);
      const float mb = rbf(mean), den = rbf(sqrtf(rbf(rbf(var) + 1e-5f)));
#pragma unroll
      for (int u = 0; u < 4; ++u) xn[r][u] = pk2(rbf(e[2 * u] - mb) / den, rbf(e[2 * u + 1] - mb) / den);
      if (lane < 56) *(u32x4*)(lds + L_XN + r * 896 + lane * 16) = xn[r];
    }
    asm volatile("s_waitcnt lgkmcnt(0)" ::: "memory");
    u32 xp0[7], xp1[7];
#pragma unroll
    for (int i = 0; i < 7; ++i) { xp0[i] = *(const u32*)(lds + L_XN + (rg + 32 * i) * 4); xp1[i] = *(const u32*)(lds + L_XN + 896 + (rg + 32 * i) * 4); }
    {
      float v[16], o[4];
#pragma unroll
      for (int i = 0; i < 16; ++i) v[i] = 0.f;
      gemv_pairs(w, xp0, v); gemv_pairs(w, xp1, v + 8);
      reduce16(v, o);
      if (c < 2) {
        const int col = jj * 16 + half * 8 + 4 * (g & 1);
        st_sc1(u32x4{pk2(o[0], o[1]), tag, pk2(o[2], o[3]), tag}, rws, WS_Q + ((g >> 1) * 8 + xl) * 2048 + col * 4);
      }
    }
    {
      u32x4 qa, qb;
      for (u32 spins = 0; spins < SPIN_CAP; ++spins) {
        poll2(qa, qb, rws, WS_Q + grp * 2048 + lane * 16);
        if (__all(tags_ok(qa, tag) && tags_ok(qb, tag))) break;
        __builtin_amdgcn_s_sleep(1);
      }
      *(u32x2*)(lds + L_Q + lane * 8) = u32x2{qa.x, qa.z};
      *(u32x2*)(lds + L_Q + 512 + lane * 8) = u32x2{qb.x, qb.z};
      if (lane == 0) *(u32*)(lds + L_TAG) = tag;
    }
    wg_barrier();
    __builtin_amdgcn_s_setprio(0);
    const int li = kvh * 2 + (jj & 1), f = xl * 14 + li;
    if (li < 14) {
      u32 xp[7];
#pragma unroll
      for (int i = 0; i < 7; ++i) xp[i] = b ? xp1[i] : xp0[i];
      float v[16], o[4];
#pragma unroll
      for (int i = 0; i < 16; ++i) v[i] = 0.f;
      const int vo = rg * 2 * (FF * 2) + (f * 16 + half * 8) * 2;
      const rsrc_t r1 = mkrsrc(p.w1, DM * FF * 2), r3 = mkrsrc(p.w3, DM * FF * 2), r2 = mkrsrc(p.w2, FF * DM * 2);
      u32x4 w3r[14];
#pragma unroll
      for (int i = 0; i < 7; ++i) { w[2 * i] = bload(r1, vo, i * 64 * FF * 2); w[2 * i + 1] = bload(r1, vo + FF * 2, i * 64 * FF * 2); }
#pragma unroll
      for (int i = 0; i < 7; ++i) { w3r[2 * i] = bload(r3, vo, i * 64 * FF * 2); w3r[2 * i + 1] = bload(r3, vo + FF * 2, i * 64 * FF * 2); }
      u32x4 w2r[16];
#pragma unroll
      for (int i = 0; i < 16; ++i) w2r[i] = bload(r2, lane * 16, (f * 16 + i) * DM * 2);
      gemv_pairs(w, xp, v); gemv_pairs(w3r, xp, v + 8);
      reduce16(v, o);
      float hh[4];
#pragma unroll
      for (int i = 0; i < 4; ++i) {
        auto sw = __builtin_amdgcn_permlane32_swap(fbits(o[i]), fbits(o[i]), false, false);
        const float gt = rbf(bitsf(sw[0])), up = rbf(bitsf(sw[1]));
        const float sl = rbf(gt / (1.0f + __expf(-gt)));
        hh[i] = sl * up;
      }
      const u32 h01 = pk2(hh[0], hh[1]), h23 = pk2(hh[2], hh[3]);
      float a[8];
#pragma unroll
      for (int i = 0; i < 8; ++i) a[i] = 0.f;
#pragma unroll
      for (int i = 0; i < 8; ++i) {
        const u32 hp = __builtin_amdgcn_readlane((i & 1) ? h23 : h01, ((i >> 1) & 1) * 16 + (i >> 2));
#pragma unroll
        for (int u = 0; u < 4; ++u) {
          const u32 lo = __builtin_amdgcn_perm(w2r[2 * i + 1][u], w2r[2 * i][u], 0x05040100u);
          const u32 hi = __builtin_amdgcn_perm(w2r[2 * i + 1][u], w2r[2 * i][u], 0x07060302u);
          a[2 * u] = dot2(lo, hp, a[2 * u]); a[2 * u + 1] = dot2(hi, hp, a[2 * u + 1]);
        }
      }
      if (lane < 56) {
        const int o4 = WS_P4 + ((b * 112 + 2 * lane) * 128 + f) * 32;
        st_sc1(u32x4{fbits(a[0]), tag, fbits(a[1]), tag}, rws, o4);
        st_sc1(u32x4{fbits(a[2]), tag, fbits(a[3]), tag}, rws, o4 + 16);
        st_sc1(u32x4{fbits(a[4]), tag, fbits(a[5]), tag}, rws, o4 + 4096);
        st_sc1(u32x4{fbits(a[6]), tag, fbits(a[7]), tag}, rws, o4 + 4096 + 16);
      }
    }
    u32x4 wo[8][4];
    {
      const rsrc_t ro = mkrsrc(p.wo, QN * DM * 2);
      const int vo = (kvh * 512 + s * 32 + 8 * g) * (DM * 2) + c * 16;
#pragma unroll
      for (int R = 0; R < 8; ++R)
#pragma unroll
        for (int C = 0; C < 4; ++C) wo[R][C] = bload(ro, vo + C * 256, R * DM * 2);
    }
    float fb0 = 0.f, fb1 = 0.f;
    u32 xres = 0;
    if (li < 14) {
      xres = __builtin_amdgcn_raw_buffer_load_b32(rx, (b * DM + f * 4 + (lane & 1) * 2) * 2, 0, 0);
      __builtin_amdgcn_s_sleep(127); __builtin_amdgcn_s_sleep(127); __builtin_amdgcn_s_sleep(127);
      gather4<112>(rws, WS_P4 + (b * 112 + f) * 4096, lane, tag, fb0, fb1);
    }
    wg_barrier();
    float ov[8];
    {
      const int pr = c;
      u32x4 ml, og[4];
      const int mlo = WS_ML + ((grp * 16 + s) * 16 + pr) * 16, po = WS_P2 + ((grp * 16 + s) * 16 + pr) * 256 + g * 64;
      for (u32 spins = 0; spins < SPIN_CAP; ++spins) {
        poll1(ml, rws, mlo);
        if (__all(tags_ok(ml, tag))) break;
        __builtin_amdgcn_s_sleep(2);
      }
      for (u32 spins = 0; spins < SPIN_CAP; ++spins) {
        poll4<16>(og, rws, po);
        if (__all(tags_ok(og[0], tag) && tags_ok(og[1], tag) && tags_ok(og[2], tag) && tags_ok(og[3], tag))) break;
        __builtin_amdgcn_s_sleep(1);
      }
      const float mp = bitsf(ml.x), M = row_max(mp), wgt = __builtin_amdgcn_exp2f(mp - M);
      const float inv = 1.0f / row_sum(wgt * bitsf(ml.z));
#pragma unroll
      for (int k = 0; k < 4; ++k) { ov[2 * k] = row_sum(wgt * bitsf(og[k].x)) * inv; ov[2 * k + 1] = row_sum(wgt * bitsf(og[k].z)) * inv; }
    }
    f32x4 oc[4];
#pragma unroll
    for (int C = 0; C < 4; ++C) oc[C] = f32x4{0.f, 0.f, 0.f, 0.f};
#pragma unroll
    for (int R = 0; R < 8; ++R) {
      const u32 pp = pk2(ov[R], ov[R]);
      const u32x4 A = {pp & msk[0], pp & msk[1], pp & msk[2], pp & msk[3]};
#pragma unroll
      for (int C = 0; C < 4; ++C) oc[C] = mfma(A, wo[R][C], oc[C]);
    }
    {
      const int pidx = kvh * 16 + s;
      const f32x4 e0 = (g & 1) ? oc[2] : oc[0], e1 = (g & 1) ? oc[3] : oc[1];
      const int cg0 = (g & 1) * 64 + 2 * c + (g >> 1), cg1 = cg0 + 32;
      const int o0 = WS_P3 + ((b * 112 + cg0) * 128 + pidx) * 32;
      st_sc1(u32x4{fbits(e0[0]), tag, fbits(e0[1]), tag}, rws, o0);
      st_sc1(u32x4{fbits(e0[2]), tag, fbits(e0[3]), tag}, rws, o0 + 16);
      if (cg1 < 112) {
        st_sc1(u32x4{fbits(e1[0]), tag, fbits(e1[1]), tag}, rws, o0 + 32 * 4096);
        st_sc1(u32x4{fbits(e1[2]), tag, fbits(e1[3]), tag}, rws, o0 + 32 * 4096 + 16);
      }
    }
    if (li < 14) {
      float a0, a1;
      gather4<128>(rws, WS_P3 + (b * 112 + f) * 4096, lane, tag, a0, a1);
      const float r0 = rbf(lo16(xres) + rbf(a0)) + rbf(fb0), r1 = rbf(hi16(xres) + rbf(a1)) + rbf(fb1);
      if (lane < 2) *(u32*)(p.out + b * DM + f * 4 + lane * 2) = pk2(r0, r1);
      if (bid == 0 && lane == 0) __builtin_amdgcn_raw_buffer_store_b32(tag, rws, WS_E, 0, 16);
    }
  } else {
    const int w = wave - 1;
    const rsrc_t rk = mkrsrc(p.K + (size_t)grp * (SK * DH * 2), SK * DH * 2);
    const rsrc_t rv = mkrsrc(p.V + (size_t)grp * (SK * DH * 2), SK * DH * 2);
    const int ko = (c >> 2) * 512 + g * 64 + (c & 3) * 16, ko2 = ko + 4096;
    const int vo = g * 512 + c * 16, vo2 = vo + 4096;
    u32x4 kb[2][8], vb[2][8];
#define LD(dst, rs, voff, so, imm) asm volatile("buffer_load_dwordx4 %0, %1, %2, %3 offen offset:" #imm " nt" : "=v"(dst) : "v"(voff), "s"(rs), "s"(so))
#define ISSUE8(B, rs, o1, o2, so) do { LD(B[0], rs, o1, so, 0); LD(B[1], rs, o1, so, 256); LD(B[2], rs, o1, so, 2048); LD(B[3], rs, o1, so, 2304); \
    LD(B[4], rs, o2, so, 0); LD(B[5], rs, o2, so, 256); LD(B[6], rs, o2, so, 2048); LD(B[7], rs, o2, so, 2304); __builtin_amdgcn_sched_barrier(0); } while (0)
#define WAIT8(B, N) do { asm volatile("s_waitcnt vmcnt(" #N ")" : "+v"(B[0]), "+v"(B[1]), "+v"(B[2]), "+v"(B[3]), "+v"(B[4]), "+v"(B[5]), "+v"(B[6]), "+v"(B[7])); \
    __builtin_amdgcn_sched_barrier(0); } while (0)
    const int so0 = (s * TILES_WG + w) * 8192;
    constexpr int SSTEP = NWS * 8192, SO_NONE = 0x7f000000;
    ISSUE8(kb[0], rk, ko, ko2, so0);
    ISSUE8(vb[0], rv, vo, vo2, so0);
    asm volatile("s_waitcnt vmcnt(8)" ::: "memory");
    { const int so1 = so0 + SSTEP; ISSUE8(kb[1], rk, ko, ko2, so1); ISSUE8(vb[1], rv, vo, vo2, so1); }
    wg_barrier();
    const int hq = (c >> 2) & 1;
    const u32x4 qf0 = *(const u32x4*)(lds + L_Q + hq * 512 + g * 64 + (c & 3) * 16);
    const u32x4 qf1 = *(const u32x4*)(lds + L_Q + hq * 512 + 256 + g * 64 + (c & 3) * 16);
    const u32 tag = *(const u32*)(lds + L_TAG);
    float m = -INFINITY, mc = -INFINITY, l = 0.f;
    f32x4 acc0 = {0.f, 0.f, 0.f, 0.f}, acc1 = {0.f, 0.f, 0.f, 0.f};
#pragma nounroll
    for (int i = 0; i < TPW; i += 2) {
#pragma unroll
      for (int d = 0; d < 2; ++d) {
        const int son = (i + d + 2 < TPW) ? so0 + (i + d + 2) * SSTEP : SO_NONE;
        WAIT8(kb[d], 24);
        f32x4 dd[4];
#pragma unroll
        for (int t = 0; t < 4; ++t) dd[t] = mfma(kb[d][2 * t], qf0, f32x4{0.f, 0.f, 0.f, 0.f});
#pragma unroll
        for (int t = 0; t < 4; ++t) dd[t] = mfma(kb[d][2 * t + 1], qf1, dd[t]);
        __builtin_amdgcn_sched_barrier(0);
        ISSUE8(kb[d], rk, ko, ko2, son);
        float sc[4];
#pragma unroll
        for (int t = 0; t < 4; ++t) sc[t] = ((dpp<0x00>(dd[t][0]) + dpp<0x55>(dd[t][1])) + dpp<0xAA>(dd[t][2])) + dpp<0xFF>(dd[t][3]);
        { const u32 a = pk2(sc[0], sc[1]), bb = pk2(sc[2], sc[3]); sc[0] = lo16(a); sc[1] = hi16(a); sc[2] = lo16(bb); sc[3] = hi16(bb); }
        const float mx = fmaxf(fmaxf(sc[0], sc[1]), fmaxf(sc[2], sc[3]));
        if (__any(mx > m)) {
          const float mn = fmaxf(m, xrow_max(mx)), mcn = mn * SC2;
          const float al = __builtin_amdgcn_exp2f(mc - mcn);
          const float a0 = bitsf(__builtin_amdgcn_readlane(fbits(al), 0)), a1 = bitsf(__builtin_amdgcn_readlane(fbits(al), 4));
          const float aa = (g & 1) ? a1 : a0;
          l *= al; acc0 *= aa; acc1 *= aa; m = mn; mc = mcn;
        }
        u32x4 A[4];
#pragma unroll
        for (int t = 0; t < 4; ++t) {
          const float pt = __builtin_amdgcn_exp2f(fmaf(sc[t], SC2, -mc));
          l += pt;
          const u32 pp = pk2(pt, pt);
          A[t] = u32x4{pp & msk[0], pp & msk[1], pp & msk[2], pp & msk[3]};
        }
        __builtin_amdgcn_sched_barrier(0);
        WAIT8(vb[d], 24);
#pragma unroll
        for (int t = 0; t < 4; ++t) { acc0 = mfma(A[t], vb[d][2 * t], acc0); acc1 = mfma(A[t], vb[d][2 * t + 1], acc1); }
        __builtin_amdgcn_sched_barrier(0);
        ISSUE8(vb[d], rv, vo, vo2, son);
      }
    }
    WAIT8(kb[0], 0); WAIT8(vb[0], 0); WAIT8(kb[1], 0); WAIT8(vb[1], 0);
    l = xrow_sum(l);
    float* lo_ = (float*)(lds + L_O) + w * 512 + (g & 1) * 256 + 8 * c + 4 * (g >> 1);
    *(f32x4*)lo_ = acc0; *(f32x4*)(lo_ + 128) = acc1;
    if (lane == 0 || lane == 4) *(f32x2*)(lds + L_ML + (w * 2 + (lane >> 2)) * 8) = f32x2{mc, l};
    wg_barrier();
    const int t = tid - 64, h = t >> 7, d0 = 2 * (t & 127);
    f32x2 mlw[NWS], ow[NWS];
#pragma unroll
    for (int k = 0; k < NWS; ++k) { mlw[k] = *(const f32x2*)(lds + L_ML + (k * 2 + h) * 8); ow[k] = *(const f32x2*)(lds + L_O + (k * 512 + h * 256 + d0) * 4); }
    float M = mlw[0].x;
#pragma unroll
    for (int k = 1; k < NWS; ++k) M = fmaxf(M, mlw[k].x);
    float L = 0.f, o0 = 0.f, o1 = 0.f;
#pragma unroll
    for (int k = 0; k < NWS; ++k) { const float wt = __builtin_amdgcn_exp2f(mlw[k].x - M); L = fmaf(wt, mlw[k].y, L); o0 = fmaf(wt, ow[k].x, o0); o1 = fmaf(wt, ow[k].y, o1); }
    const int cons = h * 8 + (d0 >> 5);
    st_sc1(u32x4{fbits(o0), tag, fbits(o1), tag}, rws, WS_P2 + ((grp * 16 + cons) * 16 + s) * 256 + (d0 & 31) * 8);
    if ((t & 15) == 0) st_sc1(u32x4{fbits(M), tag, fbits(L), tag}, rws, WS_ML + ((grp * 16 + cons) * 16 + s) * 16);
  }
}

extern "C" void kernel_launch(void* const* d_in, const int* in_sizes, int n_in, void* d_out, int out_size, void* d_ws, size_t ws_size,
                              hipStream_t stream) {
  Params p;
  p.x = (const u16*)d_in[0]; p.K = (const char*)d_in[1]; p.V = (const char*)d_in[2]; p.wq = (const u16*)d_in[3]; p.wo = (const u16*)d_in[4];
  p.w1 = (const u16*)d_in[6]; p.w3 = (const u16*)d_in[7]; p.w2 = (const u16*)d_in[8]; p.out = (u16*)d_out; p.ws = (char*)d_ws;
  fused_decode_kernel<<<NWG, NTHR, 0, stream>>>(p);
}
```

```cpp
#include <hip/hip_runtime.h>
#include <hip/hip_bf16.h>
#include <stdint.h>

typedef unsigned u32;
typedef unsigned short u16;
typedef unsigned u32x4 __attribute__((ext_vector_type(4)));
typedef unsigned u32x2 __attribute__((ext_vector_type(2)));
typedef unsigned u32x16 __attribute__((ext_vector_type(16)));
typedef float f32x4 __attribute__((ext_vector_type(4)));
typedef float f32x2 __attribute__((ext_vector_type(2)));
typedef __bf16 bf16x8 __attribute__((ext_vector_type(8)));
typedef __bf16 bf16x2 __attribute__((ext_vector_type(2)));
typedef __amdgpu_buffer_rsrc_t rsrc_t;

#define CAP 4
#define CAPM CAP
#define CAPB 6
#define NBOOST 2
#define RW 2
#define PRE 4
#define HOT 4
#define SCHED0 700
#define SCHEDT 448
#define QGAP 16
#define ML_THR 12

constexpr int DM = 448, SK = 32768, DH = 256, FF = 1792, QN = 4096;
constexpr int NWS = 4;
constexpr int NTHR = (NWS + 1) * 64;
constexpr int NWG = 256;
constexpr int TPW = SK / 16 / 16 / NWS;
constexpr u32 SPIN_CAP = 1u << 15;
constexpr float SC2 = 0.0625f * 1.4426950408889634f;

constexpr int WS_E = 0;
constexpr int WS_FL = 1024;
constexpr int WS_H = 4096;
constexpr int WS_Q = WS_H + 2 * 896 * 8 + 2048;
constexpr int WS_G = WS_Q + 2 * 8 * 2 * 4096;
constexpr int WS_ML = WS_G + 28 * 8192;
constexpr int WS_P2 = WS_ML + 16 * 16 * 16 * 16;
constexpr int WS_P3 = WS_P2 + 16 * 16 * 16 * 256;
constexpr int P3_ROW = DM * 8;
constexpr int WS_END = WS_P3 + 2 * 128 * P3_ROW;
constexpr int OOB = 0x7f000000;

constexpr int L_XN = 0;
constexpr int L_Q = 2048;
constexpr int L_TAG = 3072;
constexpr int L_ML = 3328;
constexpr int L_O = 4096;
constexpr int L_S = 16384;
constexpr int L_BYTES = 96 * 1024;

struct Params {
  const char* K; const char* V; const u16* wq; const u16* w1; const u16* w3; char* ws;
  const u16* x; const u16* wo; const u16* w2; u16* out;
};

__device__ __forceinline__ float lo16(u32 v) { return __builtin_bit_cast(float, v << 16); }
__device__ __forceinline__ float hi16(u32 v) { return __builtin_bit_cast(float, v & 0xffff0000u); }
__device__ __forceinline__ u32 pk2(float a, float b) { f32x2 v = {a, b}; return __builtin_bit_cast(u32, __builtin_convertvector(v, bf16x2)); }
__device__ __forceinline__ float rbf(float a) { return lo16(pk2(a, a)); }
__device__ __forceinline__ u32 fbits(float a) { return __builtin_bit_cast(u32, a); }
__device__ __forceinline__ float bitsf(u32 a) { return __builtin_bit_cast(float, a); }
__device__ __forceinline__ float dot2(u32 a, u32 b, float c) {
  return __builtin_amdgcn_fdot2_f32_bf16(__builtin_bit_cast(bf16x2, a), __builtin_bit_cast(bf16x2, b), c, false);
}
__device__ __forceinline__ f32x4 mfma(u32x4 a, u32x4 b, f32x4 c) {
  return __builtin_amdgcn_mfma_f32_16x16x32_bf16(__builtin_bit_cast(bf16x8, a), __builtin_bit_cast(bf16x8, b), c, 0, 0, 0);
}
template <int CTRL> __device__ __forceinline__ float dpp(float x) {
  return __builtin_bit_cast(float, __builtin_amdgcn_mov_dpp(__builtin_bit_cast(int, x), CTRL, 0xf, 0xf, true));
}
constexpr int ROR1 = 0x121, ROR2 = 0x122, ROR4 = 0x124, ROR8 = 0x128;
__device__ __forceinline__ float row_sum(float x) { x += dpp<ROR8>(x); x += dpp<ROR4>(x); x += dpp<ROR2>(x); x += dpp<ROR1>(x); return x; }
__device__ __forceinline__ float row_max(float x) { x = fmaxf(x, dpp<ROR8>(x)); x = fmaxf(x, dpp<ROR4>(x)); x = fmaxf(x, dpp<ROR2>(x)); x = fmaxf(x, dpp<ROR1>(x)); return x; }
__device__ __forceinline__ float swap16_add(float a, float b) {
  auto s = __builtin_amdgcn_permlane16_swap(fbits(a), fbits(b), false, false); return bitsf(s[0]) + bitsf(s[1]);
}
__device__ __forceinline__ float swap32_add(float a, float b) {
  auto s = __builtin_amdgcn_permlane32_swap(fbits(a), fbits(b), false, false); return bitsf(s[0]) + bitsf(s[1]);
}
__device__ __forceinline__ float xrow_sum(float x) { x = swap16_add(x, x); return swap32_add(x, x); }
__device__ __forceinline__ float xrow_max(float x) {
  auto s = __builtin_amdgcn_permlane16_swap(fbits(x), fbits(x), false, false); x = fmaxf(bitsf(s[0]), bitsf(s[1]));
  auto t = __builtin_amdgcn_permlane32_swap(fbits(x), fbits(x), false, false); return fmaxf(bitsf(t[0]), bitsf(t[1]));
}
__device__ __forceinline__ rsrc_t mkrsrc(const void* p, int bytes) { return __builtin_amdgcn_make_buffer_rsrc((void*)p, 0, bytes, 0x00020000); }
__device__ __forceinline__ u32x4 bload(rsrc_t r, int voff, int soff) { return __builtin_amdgcn_raw_buffer_load_b128(r, voff, soff, 2); }
__device__ __forceinline__ void st_sc1(u32x4 v, rsrc_t r, int voff) { __builtin_amdgcn_raw_buffer_store_b128(v, r, voff, 0, 16); }
__device__ __forceinline__ void wg_barrier() {
  asm volatile("s_waitcnt lgkmcnt(0)\n\ts_barrier" ::: "memory");
}
__device__ __forceinline__ bool tags_ok(u32x4 a, u32 tag) { return a.y == tag && a.w == tag; }
__device__ __forceinline__ u32 tagdiff(u32x4 a, u32 tag) { return __builtin_amdgcn_bitop3_b32(a.y, a.w, tag, 0x7E); }
__device__ __forceinline__ bool none(u32 x) { return __builtin_amdgcn_ballot_w64(x != 0u) == 0ull; }
#define LDG(dst, rs, voff, so, imm, pol) asm volatile("buffer_load_dwordx4 %0, %1, %2, %3 offen offset:" #imm pol : "=v"(dst) : "v"(voff), "s"(rs), "s"(so))
#define LDG2(dst, rs, voff, so, imm, pol) asm volatile("buffer_load_dwordx2 %0, %1, %2, %3 offen offset:" #imm pol : "=v"(dst) : "v"(voff), "s"(rs), "s"(so))
#define TIE4(B) "+v"((B)[0]), "+v"((B)[1]), "+v"((B)[2]), "+v"((B)[3])
#define TIE8(B) TIE4(B), TIE4((B) + 4)
#define WAIT8(B, N) do { asm volatile("s_waitcnt vmcnt(%8)" : TIE8(B) : "i"(N) : "memory"); __builtin_amdgcn_sched_barrier(0); } while (0)
#define POLL8(B, rs, voff, so0, so1) do { LDG(B[0], rs, voff, so0, 0, " sc1"); LDG(B[1], rs, voff, so0, 1024, " sc1"); LDG(B[2], rs, voff, so0, 2048, " sc1"); LDG(B[3], rs, voff, so0, 3072, " sc1"); \
    LDG(B[4], rs, voff, so1, 0, " sc1"); LDG(B[5], rs, voff, so1, 1024, " sc1"); LDG(B[6], rs, voff, so1, 2048, " sc1"); LDG(B[7], rs, voff, so1, 3072, " sc1"); WAIT8(B, 0); } while (0)
#define DIFF4(B, tag) (tagdiff((B)[0], tag) | tagdiff((B)[1], tag) | tagdiff((B)[2], tag) | tagdiff((B)[3], tag))
#define DIFF8(B, tag) (DIFF4(B, tag) | DIFF4((B) + 4, tag))
#define SPIN _Pragma("nounroll") for (u32 spins = 0; spins < SPIN_CAP; ++spins)

__device__ __forceinline__ u32x4 layer_norm(u32x4 xr, int lane) {
  float e[8];
#pragma unroll
  for (int u = 0; u < 4; ++u) { e[2 * u] = lane < 56 ? lo16(xr[u]) : 0.f; e[2 * u + 1] = lane < 56 ? hi16(xr[u]) : 0.f; }
  const float sm = ((e[0] + e[1]) + (e[2] + e[3])) + ((e[4] + e[5]) + (e[6] + e[7]));
  const float mean = xrow_sum(row_sum(sm)) * (1.0f / DM);
  float sq = 0.f;
#pragma unroll
  for (int k = 0; k < 8; ++k) { const float d = e[k] - mean; sq = fmaf(d, d, sq); }
  sq = lane < 56 ? sq : 0.f;
  const float var = xrow_sum(row_sum(sq)) * (1.0f / DM);
  const float mb = rbf(mean), den = rbf(sqrtf(rbf(rbf(var) + 1e-5f)));
  const float rc = __builtin_amdgcn_rcpf(den);
  float y[8];
#pragma unroll
  for (int k = 0; k < 8; ++k) { const float a = rbf(e[k] - mb), q = a * rc; y[k] = fmaf(fmaf(-q, den, a), rc, q); }
  return u32x4{pk2(y[0], y[1]), pk2(y[2], y[3]), pk2(y[4], y[5]), pk2(y[6], y[7])};
}

__device__ __forceinline__ void final_reduce(rsrc_t rws, rsrc_t rout, u32 tag, u32 xres, int fb, int fcg, int lane) {
  u32x4 t[16];
  const int vo = WS_P3 + (fb * 128 + (lane >> 3)) * P3_ROW + fcg * 128 + (lane & 7) * 16;
  u32 todo = 0xfu;
#pragma unroll
  for (int k = 0; k < 16; ++k) t[k] = u32x4{0u, 0u, 0u, 0u};
  const int vo1 = vo + 8 * P3_ROW, vo2 = vo + 16 * P3_ROW, vo3 = vo + 24 * P3_ROW;
  SPIN {
    u32 tds;
    asm volatile("s_nop 0\n\tv_readfirstlane_b32 %0, %1" : "=s"(tds) : "v"(todo));
#pragma unroll
    for (int k = 0; k < 4; ++k) {
      const int so = k * 32 * P3_ROW;
      asm volatile("s_bitcmp0_b32 %10, %11\n\ts_cbranch_scc1 1f\n\tbuffer_load_dwordx4 %0, %4, %8, %9 offen sc1\n\tbuffer_load_dwordx4 %1, %5, %8, %9 offen sc1\n\t"
                   "buffer_load_dwordx4 %2, %6, %8, %9 offen sc1\n\tbuffer_load_dwordx4 %3, %7, %8, %9 offen sc1\n1:"
                   : TIE4(t + 4 * k) : "v"(vo), "v"(vo1), "v"(vo2), "v"(vo3), "s"(rws), "s"(so), "s"(tds), "i"(k) : "scc");
    }
    asm volatile("s_waitcnt vmcnt(0)" : TIE8(t), TIE8(t + 8) :: "memory");
    const u32 b0 = DIFF4(t, tag), b1 = DIFF4(t + 4, tag), b2 = DIFF4(t + 8, tag), b3 = DIFF4(t + 12, tag);
    todo = (none(b0) ? 0u : 1u) | (none(b1) ? 0u : 2u) | (none(b2) ? 0u : 4u) | (none(b3) ? 0u : 8u);
    if (todo == 0u) break;
  }
  f32x2 sm[16];
#pragma unroll
  for (int k = 0; k < 16; ++k) sm[k] = f32x2{bitsf(t[k].x), bitsf(t[k].z)};
#pragma unroll
  for (int h = 8; h >= 1; h >>= 1)
#pragma unroll
    for (int k = 0; k < h; ++k) sm[k] += sm[k + h];
  float a0 = sm[0].x, a1 = sm[0].y;
  a0 += dpp<ROR8>(a0); a1 += dpp<ROR8>(a1);
  a0 = xrow_sum(a0); a1 = xrow_sum(a1);
  __builtin_amdgcn_raw_buffer_store_b32(pk2(lo16(xres) + a0, hi16(xres) + a1), rout, lane < 8 ? (fb * DM + fcg * 16 + lane * 2) * 2 : OOB, 0, 0);
}

__global__ void __launch_bounds__(NTHR, 2) fused_decode_kernel(Params args) {
  __shared__ __attribute__((aligned(16))) char lds[L_BYTES];
  u32x16 ka; u32x4 kb; u32x2 t0;
  asm volatile("s_load_dwordx16 %0, %3, 0x0\n\ts_load_dwordx4 %1, %3, 0x40\n\ts_memrealtime %2" : "=&s"(ka), "=&s"(kb), "=&s"(t0) : "s"(__builtin_amdgcn_kernarg_segment_ptr()));
  const int tid = threadIdx.x, lane = tid & 63, wave = __builtin_amdgcn_readfirstlane(tid >> 6);
  const int bid = blockIdx.x, xl = bid & 7, jj = bid >> 3;
  const int s = xl * 2 + (jj & 1), kvh = (jj >> 1) & 7, b = jj >> 4, grp = b * 8 + kvh;
  const int g = lane >> 4, c = lane & 15;
  const int fidx = (jj >> 1) * 8 + xl, kq = fidx / 28, fs = fidx - 28 * kq;
  const bool isq = (jj & 1) == 0, isproj = isq || fidx < 112;
  const int w = wave - 1;
  const int ridx = (jj >> 2) * 8 + xl;
  const bool isfin = (jj & 3) == 1 && ridx < 56;
  const int fb = ridx >= 28 ? 1 : 0, fcg = ridx - 28 * fb;
  const int pair0 = isq ? ((jj >> 1) & 1) * 112 + 28 * w : kq * 56 + 28 * (w & 1);
  const int ko = (c >> 2) * 512 + g * 64 + (c & 3) * 16;
  const int so0 = ((s / RW) * (RW * TPW * NWS) + (s % RW) * NWS) * 8192 + w * 2048;
  const int wld = isq ? QN * 2 : FF * 2;
  const int qo = (lane >> 3) * 2 * wld + (isq ? xl * 8 + (jj >> 2) : fs) * 128 + (lane & 7) * 16, qo2 = qo + wld;
  const int qo3 = lane < 32 ? qo : OOB, qo4 = lane < 32 ? qo2 : OOB;
  const int wso = pair0 * 2 * wld, wst = 16 * wld, xo = lane * 16;
  asm volatile("s_waitcnt lgkmcnt(0)" : "+s"(ka), "+s"(kb), "+s"(t0) : "v"(ko), "s"(so0), "v"(qo), "v"(qo2), "v"(qo3), "v"(qo4), "s"(wso), "s"(wst), "v"(xo), "s"(grp));
  const u32 tick0 = t0.x;
#define ARGP(v, i) ((char*)(((unsigned long long)(v)[(i) + 1] << 32) | (v)[i]))
  Params p;
  p.K = ARGP(ka, 0); p.V = ARGP(ka, 2); p.wq = (const u16*)ARGP(ka, 4); p.w1 = (const u16*)ARGP(ka, 6); p.w3 = (const u16*)ARGP(ka, 8); p.ws = ARGP(ka, 10);
  p.x = (const u16*)ARGP(ka, 12); p.wo = (const u16*)ARGP(ka, 14); p.w2 = (const u16*)ARGP(kb, 0); p.out = (u16*)ARGP(kb, 2);
  const rsrc_t rws = mkrsrc(p.ws, WS_END);
  const rsrc_t rx = mkrsrc(p.x, 2 * DM * 2);
  const rsrc_t rk = mkrsrc(p.K + (size_t)grp * (SK * DH * 2), SK * DH * 2);
  u32x4 wr[8], xr[2];
  u32 e_old;
  {
    const int zero = 0;
    if (wave == 0) {
      LDG(xr[0], rx, xo, zero, 0, ""); LDG(xr[1], rx, xo, zero, 896, "");
      asm volatile("buffer_load_dword %0, off, %1, %2 sc1" : "=v"(e_old) : "s"(rws), "s"(zero));
    } else if (isproj) {
      const rsrc_t rq = mkrsrc(isq ? p.wq : (w < 2 ? p.w1 : p.w3), DM * wld);
      LDG(wr[0], rq, qo, wso, 0, " nt"); LDG(wr[1], rq, qo2, wso, 0, " nt");
      LDG(wr[2], rq, qo, wso + wst, 0, " nt"); LDG(wr[3], rq, qo2, wso + wst, 0, " nt");
      LDG(wr[4], rq, qo, wso + 2 * wst, 0, " nt"); LDG(wr[5], rq, qo2, wso + 2 * wst, 0, " nt");
      LDG(wr[6], rq, qo3, wso + 3 * wst, 0, " nt"); LDG(wr[7], rq, qo4, wso + 3 * wst, 0, " nt");
    }
    asm volatile("" :: "s"(rk));
    __builtin_amdgcn_sched_barrier(0);
    asm volatile("s_barrier" ::: "memory");
    __builtin_amdgcn_sched_barrier(0);
  }

  if (wave != 0) {
    const rsrc_t rv = mkrsrc(p.V + (size_t)grp * (SK * DH * 2), SK * DH * 2);
    const int vo = g * 512 + c * 16;
    u32x4 rb[4][8];
#define LD(dst, rs, voff, so, imm) LDG(dst, rs, voff, so, imm, " nt")
#define PAIR(B, j, rs, o1, so) do { const int sj_ = (so) + (j) * 8192; LD(B[2 * (j)], rs, o1, sj_, 0); LD(B[2 * (j) + 1], rs, o1, sj_, 256); __builtin_amdgcn_sched_barrier(0); } while (0)
#define WAITN(N) asm volatile("s_waitcnt vmcnt(%0)" :: "i"(N) : "memory")
#define WAIT2(B, j, N) do { asm volatile("s_waitcnt vmcnt(%2)" : "+v"(B[2 * (j)]), "+v"(B[2 * (j) + 1]) : "i"(N) : "memory"); __builtin_amdgcn_sched_barrier(0); } while (0)
#define REFILL_(A_, B_, rs, o, so, i0, i1) asm volatile("s_cmp_lt_i32 %5, 0\n\ts_cbranch_scc1 .LBBc%=\n\ts_waitcnt vmcnt(%7)\n\ts_cmp_lt_i32 %5, %9\n\ts_cbranch_scc1 .LBBa%=\n\t" \
    "s_waitcnt vmcnt(%6)\n.LBBa%=:\n\tbuffer_load_dwordx4 %0, %2, %3, %4 offen offset:" #i0 " nt\n\ts_cbranch_scc1 .LBBb%=\n\ts_waitcnt vmcnt(%8)\n.LBBb%=:\n\t" \
    "buffer_load_dwordx4 %1, %2, %3, %4 offen offset:" #i1 " nt\n.LBBc%=:" : "+v"(A_), "+v"(B_) : "v"(o), "s"(rs), "s"(so), "s"(st), "i"(CAP), "i"(CAPB), "i"(CAPM), "i"(4 * NBOOST - 3) : "scc", "memory")
#define REFILL(B, j, rs, o1, so) do { const int sj_ = (so) + (j) * 8192; REFILL_(B[2 * (j)], B[2 * (j) + 1], rs, o1, sj_, 0, 256); __builtin_amdgcn_sched_barrier(0); } while (0)
#define WAITL(B, j, N) do { if ((N) >= CAPB + 2) WAIT2(B, j, 30); else { \
    asm volatile("s_cmp_ge_i32 %2, 0\n\ts_cbranch_scc1 .LBBw%=\n\ts_waitcnt vmcnt(%3)\n.LBBw%=:" : "+v"(B[2 * (j)]), "+v"(B[2 * (j) + 1]) : "s"(st), "i"((N) < 30 ? (N) : 30) : "scc", "memory"); \
    __builtin_amdgcn_sched_barrier(0); } } while (0)
#define RING(pos) do { const int so_ = so0 + ((pos) >> 2) * SSTEP; if ((pos) >= HOT) WAITN(CAP); PAIR(rb[(pos) >> 2], (pos) & 3, rk, ko, so_); } while (0)
    constexpr int SSTEP = RW * NWS * 8192;
#pragma unroll
    for (int pos = 0; pos < PRE; ++pos) RING(pos);
    if (isproj) {
      wg_barrier();
      u32 x0[4], x1[4];
#pragma unroll
      for (int i = 0; i < 4; ++i) {
        const int pi = (pair0 + 8 * i + (lane >> 3)) * 4;
        x0[i] = *(const u32*)(lds + L_XN + pi); x1[i] = *(const u32*)(lds + L_XN + 896 + pi);
      }
      if (lane >= 32) { x0[3] = 0u; x1[3] = 0u; }
      WAIT8(wr, 2 * PRE);
      float v[16];
#pragma unroll
      for (int i = 0; i < 16; ++i) v[i] = 0.f;
#pragma unroll
      for (int i = 0; i < 4; ++i)
#pragma unroll
        for (int u = 0; u < 4; ++u) {
          const u32 lo = __builtin_amdgcn_perm(wr[2 * i + 1][u], wr[2 * i][u], 0x05040100u);
          const u32 hi = __builtin_amdgcn_perm(wr[2 * i + 1][u], wr[2 * i][u], 0x07060302u);
          v[2 * u] = dot2(lo, x0[i], v[2 * u]); v[2 * u + 1] = dot2(hi, x0[i], v[2 * u + 1]);
          v[8 + 2 * u] = dot2(lo, x1[i], v[8 + 2 * u]); v[8 + 2 * u + 1] = dot2(hi, x1[i], v[8 + 2 * u + 1]);
        }
      float a[8];
      f32x4 o;
#pragma unroll
      for (int i = 0; i < 8; ++i) a[i] = swap32_add(v[i], v[i + 8]);
#pragma unroll
      for (int i = 0; i < 4; ++i) { float t = swap16_add(a[i], a[i + 4]); t += dpp<ROR8>(t); o[i] = t; }
      if ((lane & 8) == 0) *(f32x4*)(lds + L_O + w * 512 + (g >> 1) * 256 + ((lane & 7) * 8 + 4 * (g & 1)) * 4) = o;
      wg_barrier();
    }
#pragma unroll
    for (int pos = PRE; pos < 16; ++pos) RING(pos);
    const int eslot = (c & 3) | ((c >> 3) << 2);
    u32 msk[4];
#pragma unroll
    for (int u = 0; u < 4; ++u) msk[u] = ((eslot >> 1) == u) ? ((eslot & 1) ? 0xffff0000u : 0x0000ffffu) : 0u;
    {
      wg_barrier();
      const int hq = (c >> 2) & 1;
      const u32x4 qf0 = *(const u32x4*)(lds + L_Q + hq * 512 + g * 64 + (c & 3) * 16);
      const u32x4 qf1 = *(const u32x4*)(lds + L_Q + hq * 512 + 256 + g * 64 + (c & 3) * 16);
      char* sp = lds + L_S + w * (TPW * 512) + lane * 8;
      float m = -INFINITY;
#pragma nounroll
      for (int i = 0; i < TPW; i += 4) {
        const bool tov = i + 4 >= TPW;
        const int st = max(SCHED0 + (i >> 2) * SCHEDT - (int)((u32)__builtin_amdgcn_s_memrealtime() - tick0), 0);
        const rsrc_t rn = tov ? rv : rk;
        const int o1 = tov ? vo : ko;
#pragma unroll
        for (int d = 0; d < 4; ++d) {
          const int son = so0 + ((i + d + 4) & (TPW - 1)) * SSTEP;
          f32x4 dd[4];
#pragma unroll
          for (int t = 0; t < 4; ++t) {
            WAIT2(rb[d], t, 30);
            dd[t] = mfma(rb[d][2 * t], qf0, f32x4{0.f, 0.f, 0.f, 0.f});
            dd[t] = mfma(rb[d][2 * t + 1], qf1, dd[t]);
            __builtin_amdgcn_sched_barrier(0);
            REFILL(rb[d], t, rn, o1, son);
          }
          float sc[4];
#pragma unroll
          for (int t = 0; t < 4; ++t) sc[t] = ((dpp<0x00>(dd[t][0]) + dpp<0x55>(dd[t][1])) + dpp<0xAA>(dd[t][2])) + dpp<0xFF>(dd[t][3]);
          const u32x2 pk = {pk2(sc[0], sc[1]), pk2(sc[2], sc[3])};
          *(u32x2*)(sp + (i + d) * 512) = pk;
          m = fmaxf(fmaxf(m, fmaxf(lo16(pk.x), hi16(pk.x))), fmaxf(lo16(pk.y), hi16(pk.y)));
          __builtin_amdgcn_sched_barrier(0);
        }
      }
      const float mc = xrow_max(m) * SC2;
      float l = 0.f;
      f32x4 acc0 = {0.f, 0.f, 0.f, 0.f}, acc1 = {0.f, 0.f, 0.f, 0.f};
#pragma nounroll
      for (int i = 0; i < TPW; i += 4) {
        const int st = min(TPW - 8 - i, max(SCHED0 + (TPW / 4 + (i >> 2)) * SCHEDT - (int)((u32)__builtin_amdgcn_s_memrealtime() - tick0), 0));
#pragma unroll
        for (int d = 0; d < 4; ++d) {
          const int son = so0 + (i + d + 4) * SSTEP;
          const u32x2 pk = *(const u32x2*)(sp + (i + d) * 512);
          const float sc[4] = {lo16(pk.x), hi16(pk.x), lo16(pk.y), hi16(pk.y)};
          u32x4 A[4];
#pragma unroll
          for (int t = 0; t < 4; ++t) {
            const float pt = __builtin_amdgcn_exp2f(fmaf(sc[t], SC2, -mc));
            l += pt;
            const u32 pp = pk2(pt, pt);
            A[t] = u32x4{pp & msk[0], pp & msk[1], pp & msk[2], pp & msk[3]};
          }
          __builtin_amdgcn_sched_barrier(0);
#pragma unroll
          for (int t = 0; t < 4; ++t) {
            WAITL(rb[d], t, 2 * (15 - 4 * d - t));
            acc0 = mfma(A[t], rb[d][2 * t], acc0); acc1 = mfma(A[t], rb[d][2 * t + 1], acc1);
            __builtin_amdgcn_sched_barrier(0);
            REFILL(rb[d], t, rv, vo, son);
          }
        }
      }
      l = xrow_sum(l);
      float* lo_ = (float*)(lds + L_O) + w * 512 + (g & 1) * 256 + 8 * c + 4 * (g >> 1);
      *(f32x4*)lo_ = acc0; *(f32x4*)(lo_ + 128) = acc1;
      if (lane == 0 || lane == 4) *(f32x2*)(lds + L_ML + (w * 2 + (lane >> 2)) * 8) = f32x2{mc, l};
      wg_barrier();
      const u32 tag = *(const u32*)(lds + L_TAG);
      const int t = tid - 64, h = t >> 7, d0 = 2 * (t & 127);
      f32x2 mlw[NWS], ow[NWS];
#pragma unroll
      for (int k = 0; k < NWS; ++k) { mlw[k] = *(const f32x2*)(lds + L_ML + (k * 2 + h) * 8); ow[k] = *(const f32x2*)(lds + L_O + (k * 512 + h * 256 + d0) * 4); }
      float M = mlw[0].x;
#pragma unroll
      for (int k = 1; k < NWS; ++k) M = fmaxf(M, mlw[k].x);
      float L = 0.f, o0 = 0.f, o1 = 0.f;
#pragma unroll
      for (int k = 0; k < NWS; ++k) { const float wt = __builtin_amdgcn_exp2f(mlw[k].x - M); L = fmaf(wt, mlw[k].y, L); o0 = fmaf(wt, ow[k].x, o0); o1 = fmaf(wt, ow[k].y, o1); }
      const int slot = (grp * 16 + h * 8 + (d0 >> 5)) * 16 + s;
      {
        const u32x4 v0 = {fbits(o0), tag, fbits(o1), tag}, v1 = {fbits(M), tag, fbits(L), tag};
        const int a0 = WS_P2 + slot * 256 + (d0 & 31) * 8, a1 = (t & 15) == 0 ? WS_ML + slot * 16 : OOB;
        asm volatile("buffer_store_dwordx4 %0, %1, %4, 0 offen sc1\n\tbuffer_store_dwordx4 %2, %3, %4, 0 offen sc1\n\ts_nop 1" :: "v"(v0), "v"(a0), "v"(v1), "v"(a1), "s"(rws) : "memory");
      }
    }
  } else {
    asm volatile("s_waitcnt vmcnt(0)\n\ts_nop 0" : "+v"(e_old), "+v"(xr[0]), "+v"(xr[1]) :: "memory");
    if (isproj) {
      const u32x4 n0 = layer_norm(xr[0], lane), n1 = layer_norm(xr[1], lane);
      if (lane < 56) { *(u32x4*)(lds + L_XN + lane * 16) = n0; *(u32x4*)(lds + L_XN + 896 + lane * 16) = n1; }
      wg_barrier();
    }
    const u32 tag = __builtin_amdgcn_readfirstlane(e_old) + 1u;
    if (lane == 0) *(u32*)(lds + L_TAG) = tag;
    if (isproj) {
      wg_barrier();
      f32x2 pa[NWS];
#pragma unroll
      for (int k = 0; k < NWS; ++k) pa[k] = *(const f32x2*)(lds + L_O + k * 512 + lane * 8);
      const f32x2 a = pa[0] + pa[1], u = pa[2] + pa[3];
      if (isq) {
        const f32x2 q = a + u;
        st_sc1(u32x4{fbits(q.x), tag, fbits(q.y), tag}, rws, WS_Q + (((lane >> 5) * 8 + xl) * 2 + ((jj >> 1) & 1)) * 4096 + (jj >> 2) * 512 + (lane & 31) * 16);
      } else {
        st_sc1(u32x4{fbits(a.x), tag, fbits(a.y), tag}, rws, WS_G + fs * 8192 + kq * 2048 + lane * 16);
        st_sc1(u32x4{fbits(u.x), tag, fbits(u.y), tag}, rws, WS_G + fs * 8192 + kq * 2048 + 1024 + lane * 16);
      }
    }
    {
      u32x4 t[8];
      const int so0 = WS_Q + grp * 8192, so1 = so0 + 4096, vo = lane * 16;
      if (!isproj) __builtin_amdgcn_s_sleep(110);
      __builtin_amdgcn_s_sleep(QGAP);
      SPIN {
        POLL8(t, rws, vo, so0, so1);
        if (none(DIFF8(t, tag))) break;
      }
#pragma unroll
      for (int k = 0; k < 4; ++k) *(u32*)(lds + L_Q + (k * 64 + lane) * 4) = pk2(bitsf(t[k].x) + bitsf(t[k + 4].x), bitsf(t[k].z) + bitsf(t[k + 4].z));
    }
    wg_barrier();
    __builtin_amdgcn_raw_buffer_store_b64(u32x2{0u, tag}, rws, lane == 0 ? WS_FL + bid * 8 : OOB, 0, 16);
    if (!isq && fidx < 28) {
      u32x4 t[8];
      const int so0 = WS_G + fs * 8192, so1 = so0 + 4096, vo = lane * 16;
      SPIN {
        POLL8(t, rws, vo, so0, so1);
        if (none(DIFF8(t, tag))) break;
        __builtin_amdgcn_s_sleep(16);
      }
      const float g0 = rbf((bitsf(t[0].x) + bitsf(t[2].x)) + (bitsf(t[4].x) + bitsf(t[6].x))), g1 = rbf((bitsf(t[0].z) + bitsf(t[2].z)) + (bitsf(t[4].z) + bitsf(t[6].z)));
      const float u0 = rbf((bitsf(t[1].x) + bitsf(t[3].x)) + (bitsf(t[5].x) + bitsf(t[7].x))), u1 = rbf((bitsf(t[1].z) + bitsf(t[3].z)) + (bitsf(t[5].z) + bitsf(t[7].z)));
      const u32 hp = pk2(rbf(g0 / (1.0f + __expf(-g0))) * u0, rbf(g1 / (1.0f + __expf(-g1))) * u1);
      __builtin_amdgcn_raw_buffer_store_b64(u32x2{hp, tag}, rws, WS_H + ((lane >> 5) * 896 + fs * 32 + (lane & 31)) * 8, 0, 16);
    }
    const int eslot = (c & 3) | ((c >> 3) << 2);
    u32 msk[4];
#pragma unroll
    for (int u = 0; u < 4; ++u) msk[u] = ((eslot >> 1) == u) ? ((eslot & 1) ? 0xffff0000u : 0x0000ffffu) : 0u;
    const int pidx = kvh * 16 + s;
    u32x2 hg[4];
    {
      __builtin_amdgcn_s_sleep(127); __builtin_amdgcn_s_sleep(127); __builtin_amdgcn_s_sleep(127);
      const int ho = WS_H + (b * 896 + pidx * 7 + (g >> 1)) * 8, zero = 0;
      SPIN {
        LDG2(hg[0], rws, ho, zero, 0, " sc1"); LDG2(hg[1], rws, ho, zero, 16, " sc1"); LDG2(hg[2], rws, ho, zero, 32, " sc1"); LDG2(hg[3], rws, ho, zero, 48, " sc1");
        asm volatile("s_waitcnt vmcnt(0)" : TIE4(hg) :: "memory");
        if (__all(hg[0].y == tag && hg[1].y == tag && hg[2].y == tag && (hg[3].y == tag || g >= 2))) break;
        __builtin_amdgcn_s_sleep(64);
      }
    }
    if (bid == NWG - 1) {
      u32x4 t[2];
      const int fo = WS_FL + lane * 16, zero = 0;
      SPIN {
        LDG(t[0], rws, fo, zero, 0, " sc1"); LDG(t[1], rws, fo, zero, 1024, " sc1");
        asm volatile("s_waitcnt vmcnt(0)" : "+v"(t[0]), "+v"(t[1]) :: "memory");
        if (none(tagdiff(t[0], tag) | tagdiff(t[1], tag))) break;
        __builtin_amdgcn_s_sleep(64);
      }
      if (lane == 0) __builtin_amdgcn_raw_buffer_store_b32(tag, rws, WS_E, 0, 16);
    }
    f32x4 of[4];
    u32x4 wo[8][4];
    {
      const rsrc_t r2 = mkrsrc(p.w2, FF * DM * 2);
      const int vo = (pidx * 14 + g) * (DM * 2) + c * 16, vo3 = c < 8 ? vo + 768 : OOB;
      u32x4 w2r[4][4];
#pragma unroll
      for (int R = 0; R < 4; ++R) {
        const bool ok = R < 3 || g < 2;
#pragma unroll
        for (int C = 0; C < 3; ++C) w2r[R][C] = bload(r2, ok ? vo + C * 256 : OOB, R * 4 * DM * 2);
        w2r[R][3] = bload(r2, ok ? vo3 : OOB, R * 4 * DM * 2);
      }
      __builtin_amdgcn_sched_barrier(0);
      const rsrc_t ro = mkrsrc(p.wo, QN * DM * 2);
      const int wvo = (kvh * 512 + s * 32 + 8 * g) * (DM * 2) + c * 16, wvo3 = c < 8 ? wvo + 768 : OOB;
#pragma unroll
      for (int R = 0; R < 8; ++R) {
#pragma unroll
        for (int C = 0; C < 3; ++C) wo[R][C] = bload(ro, wvo + C * 256, R * DM * 2);
        wo[R][3] = bload(ro, wvo3, R * DM * 2);
      }
      __builtin_amdgcn_sched_barrier(0);
#pragma unroll
      for (int C = 0; C < 4; ++C) of[C] = f32x4{0.f, 0.f, 0.f, 0.f};
#pragma unroll
      for (int R = 0; R < 4; ++R) {
        u32 pp = ((g & 1) ? hg[R].x >> 16 : hg[R].x & 0xffffu) * 0x10001u;
        if (R == 3 && g >= 2) pp = 0u;
        const u32x4 A = {pp & msk[0], pp & msk[1], pp & msk[2], pp & msk[3]};
#pragma unroll
        for (int C = 0; C < 4; ++C) of[C] = mfma(A, w2r[R][C], of[C]);
      }
      asm volatile("" : "+v"(of[0]), "+v"(of[1]), "+v"(of[2]), "+v"(of[3]));
    }
    const u32 xres = __builtin_amdgcn_raw_buffer_load_b32(rx, isfin ? (fb * DM + fcg * 16 + (lane & 7) * 2) * 2 : OOB, 0, 0);
    const rsrc_t rout = mkrsrc(p.out, 2 * DM * 2);
    {
      wg_barrier();
      const int zero = 0;
      float ov[8];
      {
        u32x4 t[5];
        const int slot = (grp * 16 + s) * 16 + c;
        const int mlo = WS_ML + slot * 16, po = WS_P2 + slot * 256 + g * 64;
        SPIN {
          LDG(t[4], rws, mlo, zero, 0, " sc1");
          asm volatile("s_waitcnt vmcnt(0)" : "+v"(t[4]) :: "memory");
          if (__builtin_popcountll(__ballot(tags_ok(t[4], tag))) >= 4 * ML_THR) break;
          __builtin_amdgcn_s_sleep(4);
        }
        SPIN {
          LDG(t[0], rws, po, zero, 0, " sc1"); LDG(t[1], rws, po, zero, 16, " sc1"); LDG(t[2], rws, po, zero, 32, " sc1"); LDG(t[3], rws, po, zero, 48, " sc1");
          LDG(t[4], rws, mlo, zero, 0, " sc1");
          asm volatile("s_waitcnt vmcnt(0)" : TIE4(t), "+v"(t[4]) :: "memory");
          if (none(tagdiff(t[0], tag) | tagdiff(t[1], tag) | tagdiff(t[2], tag) | tagdiff(t[3], tag) | tagdiff(t[4], tag))) break;
        }
        const float mp = bitsf(t[4].x), M = row_max(mp), wgt = __builtin_amdgcn_exp2f(mp - M);
        float y[9];
#pragma unroll
        for (int k = 0; k < 4; ++k) { y[2 * k] = wgt * bitsf(t[k].x); y[2 * k + 1] = wgt * bitsf(t[k].z); }
        y[8] = wgt * bitsf(t[4].z);
#pragma unroll
        for (int k = 0; k < 9; ++k) asm("" : "+v"(y[k]));
#define STEP9(CTRL) do { _Pragma("unroll") for (int k = 8; k >= 0; --k) y[k] += dpp<CTRL>(y[k]); __builtin_amdgcn_sched_barrier(0); } while (0)
        STEP9(ROR8); STEP9(ROR4); STEP9(ROR2); STEP9(ROR1);
        const float r0 = __builtin_amdgcn_rcpf(y[8]), inv = fmaf(fmaf(-y[8], r0, 1.0f), r0, r0);
#pragma unroll
        for (int k = 0; k < 8; ++k) ov[k] = y[k] * inv;
      }
      f32x4 oc[4];
#pragma unroll
      for (int C = 0; C < 4; ++C) oc[C] = of[C];
#pragma unroll
      for (int R = 0; R < 8; ++R) {
        const u32 pp = pk2(ov[R], ov[R]);
        const u32x4 A = {pp & msk[0], pp & msk[1], pp & msk[2], pp & msk[3]};
#pragma unroll
        for (int C = 0; C < 4; ++C) oc[C] = mfma(A, wo[R][C], oc[C]);
      }
      {
        const int o0 = WS_P3 + (b * 128 + pidx) * P3_ROW + (8 * c + 4 * (g >> 1) + 2 * (g & 1)) * 8;
#pragma unroll
        for (int C = 0; C < 4; ++C) {
          const float e0 = (g & 1) ? oc[C][2] : oc[C][0], e1 = (g & 1) ? oc[C][3] : oc[C][1];
          __builtin_amdgcn_raw_buffer_store_b128(u32x4{fbits(e0), tag, fbits(e1), tag}, rws, (C < 3 || c < 8) ? o0 + C * 1024 : OOB, zero, 16);
        }
      }
      if (isfin) {
        final_reduce(rws, rout, tag, xres, fb, fcg, lane);
      }
    }
  }
}

extern "C" void kernel_launch(void* const* d_in, const int* in_sizes, int n_in, void* d_out, int out_size, void* d_ws, size_t ws_size,
                              hipStream_t stream) {
  Params p;
  p.x = (const u16*)d_in[0]; p.K = (const char*)d_in[1]; p.V = (const char*)d_in[2]; p.wq = (const u16*)d_in[3]; p.wo = (const u16*)d_in[4];
  p.w1 = (const u16*)d_in[6]; p.w3 = (const u16*)d_in[7]; p.w2 = (const u16*)d_in[8]; p.out = (u16*)d_out; p.ws = (char*)d_ws;
  fused_decode_kernel<<<NWG, NTHR, 0, stream>>>(p);
}
```
